```python
import torch

def _softmax(x, axis=-1):
    return torch.softmax(x, dim=axis)
def _gelu(x):
    return torch.nn.functional.gelu(x, approximate="tanh")
def _silu(x):
    return torch.nn.functional.silu(x)
def _sigmoid(x):
    return torch.sigmoid(x)
def _layernorm(x, axis=-1, eps=1e-5):
    m = x.mean(dim=axis, keepdim=True)
    v = x.var(dim=axis, keepdim=True, unbiased=False)
    return (x - m) / torch.sqrt(v + eps)
def _scatter_add(tgt, idx, src, axis=0):
    return tgt.clone().index_add_(axis, idx, src)
def _segment_sum(src, seg, n):
    out = torch.zeros((n,) + src.shape[1:], dtype=src.dtype)
    return out.index_add_(0, seg, src)
def _pad(x, pw):
    flat = tuple(v for pair in reversed(pw) for v in pair)
    return torch.nn.functional.pad(x, flat)

kernel_name = 'gated_mix__gqa_block_cached__sliding_block_cached__decode'

def setup_inputs():

    _w = (lambda a: (lambda t: t.to(torch.bfloat16) if t.is_floating_point() else t)(torch.as_tensor(a)))
    def _A_setup():
        B, Sq, D = 2, 1, 512
        Hq, Hkv, Sk, Dh = 32, 32, 4096, 128
        assert Hq % Hkv == 0, (Hq, Hkv)
        return {
            "x": _w((lambda *s: torch.randn(*s, dtype=torch.bfloat16) * 0.05)(B, Sq, D)),
            "k_cache": _w((lambda *s: torch.randn(*s, dtype=torch.bfloat16) * 6.4)(B, Hkv, Sk, Dh)),
            "v_cache": _w((lambda *s: torch.randn(*s, dtype=torch.bfloat16) * 0.05)(B, Hkv, Sk, Dh)),
            "wq": _w((lambda *s: torch.randn(*s, dtype=torch.bfloat16) * 6.4)(D, Hq * Dh) * (D ** -0.5)),
            "wo": _w((lambda *s: torch.randn(*s, dtype=torch.bfloat16) * 0.05)(Hq * Dh, D) * ((Hq * Dh) ** -0.5)),
        }
    _A_in = _A_setup()

    def _B_setup():
        B, Sq, D = 2, 1, 512
        Hq, Hkv, Sk, Dh = 32, 32, 4096, 128
        assert Hq % Hkv == 0, (Hq, Hkv)
        return {
            "x": _w((lambda *s: torch.randn(*s, dtype=torch.bfloat16) * 0.05)(B, Sq, D)),
            "k_cache": _w((lambda *s: torch.randn(*s, dtype=torch.bfloat16) * 6.4)(B, Hkv, Sk, Dh)),
            "v_cache": _w((lambda *s: torch.randn(*s, dtype=torch.bfloat16) * 0.05)(B, Hkv, Sk, Dh)),
            "wq": _w((lambda *s: torch.randn(*s, dtype=torch.bfloat16) * 6.4)(D, Hq * Dh) * (D ** -0.5)),
            "wo": _w((lambda *s: torch.randn(*s, dtype=torch.bfloat16) * 0.05)(Hq * Dh, D) * ((Hq * Dh) ** -0.5)),
        }
    _B_in = _B_setup()

    return {
        "A_x": _A_in["x"],
        "A_k_cache": _A_in["k_cache"],
        "A_v_cache": _A_in["v_cache"],
        "A_wq": _A_in["wq"],
        "A_wo": _A_in["wo"],
        "B_x": _B_in["x"],
        "B_k_cache": _B_in["k_cache"],
        "B_v_cache": _B_in["v_cache"],
        "B_wq": _B_in["wq"],
        "B_wo": _B_in["wo"],
        "gate_w": _w((lambda *s: torch.randn(*s, dtype=torch.bfloat16) * 0.05)(512) * 16.0),
    }

def reference(A_x, A_k_cache, A_v_cache, A_wq, A_wo, B_x, B_k_cache, B_v_cache, B_wq, B_wo, gate_w):
    def _A_ref(x, k_cache, v_cache, wq, wo):
        B, Sq, D = x.shape
        Hq, Dh = 32, 128
        Hkv = k_cache.shape[1]
        rep = Hq // Hkv
        q = (lambda x, *d: x.permute(*d))((x @ wq).reshape(B, Sq, Hq, Dh), 0, 2, 1, 3)
        k = (lambda x, n, axis: torch.repeat_interleave(x, n, dim=axis))(k_cache, rep, axis=1)
        v = (lambda x, n, axis: torch.repeat_interleave(x, n, dim=axis))(v_cache, rep, axis=1)
        scores = torch.einsum("bhqd,bhkd->bhqk", q, k) * (Dh ** -0.5)
        p = _softmax(scores, axis=-1)
        o = torch.einsum("bhqk,bhkd->bhqd", p, v)
        return (lambda x, *d: x.permute(*d))(o, 0, 2, 1, 3).reshape(B, Sq, Hq * Dh) @ wo

    def _B_ref(x, k_cache, v_cache, wq, wo):
        B, Sq, D = x.shape
        Hq, Dh = 32, 128
        Hkv, Sk = k_cache.shape[1], k_cache.shape[2]
        W = min(4096, max(256, Sk // 4))
        rep = Hq // Hkv
        q = (lambda x, *d: x.permute(*d))((x @ wq).reshape(B, Sq, Hq, Dh), 0, 2, 1, 3)
        k = (lambda x, n, axis: torch.repeat_interleave(x, n, dim=axis))(k_cache[:, :, Sk - W:, :], rep, axis=1)
        v = (lambda x, n, axis: torch.repeat_interleave(x, n, dim=axis))(v_cache[:, :, Sk - W:, :], rep, axis=1)
        scores = torch.einsum("bhqd,bhkd->bhqk", q, k) * (Dh ** -0.5)
        p = _softmax(scores, axis=-1)
        o = torch.einsum("bhqk,bhkd->bhqd", p, v)
        return (lambda x, *d: x.permute(*d))(o, 0, 2, 1, 3).reshape(B, Sq, Hq * Dh) @ wo

    x = A_x
    g = _sigmoid((x * gate_w).sum(-1, keepdims=True))
    a = _A_ref(x, A_k_cache, A_v_cache, A_wq, A_wo)
    b = _B_ref(x, B_k_cache, B_v_cache, B_wq, B_wo)
    return g * a + (1 - g) * b
```

```cpp
#include <hip/hip_runtime.h>
#include <hip/hip_bf16.h>
#include <stdint.h>

typedef unsigned u32x4 __attribute__((ext_vector_type(4)));
typedef __bf16 bf16x2 __attribute__((ext_vector_type(2)));

constexpr int NB = 2, DM = 512, NH = 32, SK = 4096, DH = 128, WIN = 1024;

__device__ __forceinline__ float bf_lo(unsigned u) { return __builtin_bit_cast(float, u << 16); }
__device__ __forceinline__ float bf_hi(unsigned u) { return __builtin_bit_cast(float, u & 0xffff0000u); }
__device__ __forceinline__ float dot2(unsigned a, unsigned b, float c) {
  return __builtin_amdgcn_fdot2_f32_bf16(__builtin_bit_cast(bf16x2, a), __builtin_bit_cast(bf16x2, b), c, false);
}
template <int CTRL> __device__ __forceinline__ float dpp(float x) {
  return __builtin_bit_cast(float, __builtin_amdgcn_mov_dpp(__builtin_bit_cast(int, x), CTRL, 0xf, 0xf, true));
}

__global__ void k_q(const unsigned short* x, const unsigned short* wqA, const unsigned short* wqB, unsigned short* q) {
  int col = blockIdx.x * 64 + (threadIdx.x & 63);
  int part = threadIdx.x >> 6;
  int br = col >> 12, c = col & 4095;
  const unsigned short* w = br ? wqB : wqA;
  float a0 = 0.f, a1 = 0.f;
  for (int k = part * 128; k < part * 128 + 128; ++k) {
    float wv = bf_lo(w[(size_t)k * 4096 + c]);
    a0 += bf_lo(x[k]) * wv; a1 += bf_lo(x[512 + k]) * wv;
  }
  __shared__ float s[2][4][64];
  s[0][part][threadIdx.x & 63] = a0; s[1][part][threadIdx.x & 63] = a1;
  __syncthreads();
  if (part < 2) {
    int l = threadIdx.x & 63;
    float v = s[part][0][l] + s[part][1][l] + s[part][2][l] + s[part][3][l];
    __hip_bfloat16 r = __float2bfloat16(v);
    q[(br * 2 + part) * 4096 + c] = __builtin_bit_cast(unsigned short, r);
  }
}

__global__ void __launch_bounds__(256) k_attn(const unsigned short* q, const unsigned short* kA, const unsigned short* vA,
                       const unsigned short* kB, const unsigned short* vB, float* part) {
  int bid = blockIdx.x;
  int bh = bid / 20, ch = bid % 20;
  int br = ch >= 16;
  int key0 = br ? (SK - WIN + (ch - 16) * 256) : ch * 256;
  int b = bh >> 5, h = bh & 31;
  int lane = threadIdx.x & 63, wave = threadIdx.x >> 6, r = lane >> 4, c = lane & 15;
  const unsigned short* K = (br ? kB : kA) + ((size_t)bh * SK + key0 + wave * 64) * DH;
  const unsigned short* V = (br ? vB : vA) + ((size_t)bh * SK + key0 + wave * 64) * DH;
  u32x4 kk[16], vv[16];
#pragma unroll
  for (int i = 0; i < 16; ++i) kk[i] = __builtin_nontemporal_load((const u32x4*)(K + i * 512 + lane * 8));
#pragma unroll
  for (int i = 0; i < 16; ++i) vv[i] = __builtin_nontemporal_load((const u32x4*)(V + i * 512 + lane * 8));
  u32x4 qq = *(const u32x4*)(q + (br * 2 + b) * 4096 + h * 128 + c * 8);
  float s[16]; float m = -INFINITY;
  const float C2 = 0.088388347648318440f * 1.4426950408889634f;
#pragma unroll
  for (int i = 0; i < 16; ++i) {
    float d = dot2(kk[i][0], qq[0], 0.f); d = dot2(kk[i][1], qq[1], d); d = dot2(kk[i][2], qq[2], d); d = dot2(kk[i][3], qq[3], d);
    d += dpp<0x128>(d); d += dpp<0x124>(d); d += dpp<0x122>(d); d += dpp<0x121>(d);
    s[i] = d * C2; m = fmaxf(m, s[i]);
  }
  m = fmaxf(m, __shfl_xor(m, 16)); m = fmaxf(m, __shfl_xor(m, 32));
  float l = 0.f, acc[8] = {0,0,0,0,0,0,0,0};
#pragma unroll
  for (int i = 0; i < 16; ++i) {
    float p = __builtin_amdgcn_exp2f(s[i] - m); l += p;
#pragma unroll
    for (int j = 0; j < 4; ++j) { acc[2*j] += p * bf_lo(vv[i][j]); acc[2*j+1] += p * bf_hi(vv[i][j]); }
  }
  l += __shfl_xor(l, 16); l += __shfl_xor(l, 32);
#pragma unroll
  for (int j = 0; j < 8; ++j) { acc[j] += __shfl_xor(acc[j], 16); acc[j] += __shfl_xor(acc[j], 32); }
  float* o = part + ((size_t)bid * 4 + wave) * 132;
  if (r == 0) {
#pragma unroll
    for (int j = 0; j < 8; ++j) o[4 + c * 8 + j] = acc[j];
    if (c == 0) { o[0] = m; o[1] = l; }
  }
}

__global__ void __launch_bounds__(256) k_comb(const float* part, const unsigned short* woA, const unsigned short* woB,
                       const unsigned short* x, const unsigned short* gw, float* y) {
  int bh = blockIdx.x, b = bh >> 5, h = bh & 31, t = threadIdx.x;
  __shared__ float o[2][128]; __shared__ float red[4];
  float gs = bf_lo(x[b * 512 + t]) * bf_lo(gw[t]) + bf_lo(x[b * 512 + 256 + t]) * bf_lo(gw[256 + t]);
  for (int d = 32; d; d >>= 1) gs += __shfl_xor(gs, d);
  if ((t & 63) == 0) red[t >> 6] = gs;
  int br = t >> 7, d = t & 127;
  int n = br ? 16 : 64, base = bh * 80 + (br ? 64 : 0);
  float M = -INFINITY;
  for (int i = 0; i < n; ++i) M = fmaxf(M, part[(size_t)(base + i) * 132]);
  float L = 0.f, a = 0.f;
  for (int i = 0; i < n; ++i) { const float* p = part + (size_t)(base + i) * 132; float w = __builtin_amdgcn_exp2f(p[0] - M); L += w * p[1]; a += w * p[4 + d]; }
  __syncthreads();
  float g = 1.f / (1.f + __expf(-(red[0] + red[1] + red[2] + red[3])));
  o[br][d] = a / L * (br ? 1.f - g : g);
  __syncthreads();
  float y0 = 0.f, y1 = 0.f;
  for (int k = 0; k < 128; ++k) {
    unsigned wa = *(const unsigned*)(woA + (size_t)(h * 128 + k) * 512 + 2 * t);
    unsigned wb = *(const unsigned*)(woB + (size_t)(h * 128 + k) * 512 + 2 * t);
    y0 += o[0][k] * bf_lo(wa) + o[1][k] * bf_lo(wb);
    y1 += o[0][k] * bf_hi(wa) + o[1][k] * bf_hi(wb);
  }
  y[bh * 512 + 2 * t] = y0; y[bh * 512 + 2 * t + 1] = y1;
}
__global__ void k_fin(const float* y, __hip_bfloat16* out) {
  int i = blockIdx.x * 256 + threadIdx.x; int b = i >> 9, c = i & 511;
  float a = 0.f;
  for (int h = 0; h < 32; ++h) a += y[(b * 32 + h) * 512 + c];
  out[i] = __float2bfloat16(a);
}

extern "C" void kernel_launch(void* const* d_in, const int* in_sizes, int n_in, void* d_out, int out_size, void* d_ws, size_t ws_size, hipStream_t stream) {
  typedef const unsigned short* P;
  P x = (P)d_in[0], kA = (P)d_in[1], vA = (P)d_in[2], wqA = (P)d_in[3], woA = (P)d_in[4];
  P kB = (P)d_in[6], vB = (P)d_in[7], wqB = (P)d_in[8], woB = (P)d_in[9], gw = (P)d_in[10];
  unsigned short* q = (unsigned short*)d_ws;
  float* part = (float*)((char*)d_ws + 65536);
  float* y = (float*)((char*)d_ws + (8 << 20));
  k_q<<<128, 256, 0, stream>>>(x, wqA, wqB, q);
  k_attn<<<1280, 256, 0, stream>>>(q, kA, vA, kB, vB, part);
  k_comb<<<64, 256, 0, stream>>>(part, woA, woB, x, gw, y);
  k_fin<<<4, 256, 0, stream>>>(y, (__hip_bfloat16*)d_out);
}
```

```cpp
#include <hip/hip_runtime.h>
#include <hip/hip_bf16.h>
#include <stdint.h>

typedef unsigned u32x4 __attribute__((ext_vector_type(4)));
typedef unsigned u32x2 __attribute__((ext_vector_type(2)));
typedef float f32x4 __attribute__((ext_vector_type(4)));
typedef float f32x2 __attribute__((ext_vector_type(2)));
typedef __bf16 bf16x2 __attribute__((ext_vector_type(2)));
typedef __bf16 bf16x8 __attribute__((ext_vector_type(8)));
typedef __amdgpu_buffer_rsrc_t rsrc_t;

#define PACE 279
#define EE 24350
#define AC 18
constexpr int PACE0 = EE - 87 * PACE, PACE1 = 310;
constexpr int PW = 4;
constexpr int I0 = 2;
constexpr int QP = 6;
constexpr int QG = 14;
constexpr int AN = 16;
constexpr int N0 = 20;
constexpr int RATE = 2;
constexpr int HOP = 44;
constexpr int GTH = 62;
constexpr int WT = 70;
constexpr int EC = 1;
constexpr int ODELAY = 14, OP = 6, YDELAY = 18;
constexpr int NT_ = 2;
constexpr int DEV = 16;
constexpr int OOB = 0x7ffffff0;

constexpr unsigned ML_OFF = 4096;
constexpr unsigned T_OFF = 40960;
constexpr unsigned Q_OFF = 65536;
constexpr unsigned O_OFF = Q_OFF + 64 * 256 * 16;
constexpr unsigned Y_OFF = O_OFF + 64 * 256 * 16;
constexpr unsigned WS_USED = Y_OFF + 256 * 64 * 16;
constexpr float LOG2E = 1.4426950408889634f;

#define SB() __builtin_amdgcn_sched_barrier(0)
__device__ __forceinline__ rsrc_t mk(const void* p, unsigned n) { return __builtin_amdgcn_make_buffer_rsrc((void*)p, 0, n, 0x00020000); }
__device__ __forceinline__ float asf(unsigned u) { return __builtin_bit_cast(float, u); }
__device__ __forceinline__ unsigned asu(float f) { return __builtin_bit_cast(unsigned, f); }
__device__ __forceinline__ float bf_lo(unsigned u) { return asf(u << 16); }
__device__ __forceinline__ float bf_hi(unsigned u) { return asf(u & 0xffff0000u); }
__device__ __forceinline__ unsigned cvtpk(float a, float b) {
  f32x2 v = {a, b};
  return __builtin_bit_cast(unsigned, __builtin_convertvector(v, bf16x2));
}
__device__ __forceinline__ float rbf(float a) { return bf_lo(cvtpk(a, a)); }
__device__ __forceinline__ float dot2(unsigned a, unsigned b, float c) {
  return __builtin_amdgcn_fdot2_f32_bf16(__builtin_bit_cast(bf16x2, a), __builtin_bit_cast(bf16x2, b), c, false);
}
template <int CTRL> __device__ __forceinline__ unsigned dppu(unsigned x) { return __builtin_amdgcn_mov_dpp(x, CTRL, 0xf, 0xf, true); }
template <int CTRL> __device__ __forceinline__ float dpp(float x) { return asf(dppu<CTRL>(asu(x))); }
__device__ __forceinline__ f32x4 mfma(u32x4 a, u32x4 b, f32x4 c) {
  return __builtin_amdgcn_mfma_f32_16x16x32_bf16(__builtin_bit_cast(bf16x8, a), __builtin_bit_cast(bf16x8, b), c, 0, 0, 0);
}
__device__ __forceinline__ u32x2 swap16(unsigned a, unsigned b) { auto r = __builtin_amdgcn_permlane16_swap(a, b, false, false); return u32x2{r[0], r[1]}; }
__device__ __forceinline__ u32x2 swap32(unsigned a, unsigned b) { auto r = __builtin_amdgcn_permlane32_swap(a, b, false, false); return u32x2{r[0], r[1]}; }
__device__ __forceinline__ float rows_max(float v) {
  u32x2 t = swap16(asu(v), asu(v)); v = fmaxf(asf(t[0]), asf(t[1]));
  t = swap32(asu(v), asu(v)); return fmaxf(asf(t[0]), asf(t[1]));
}
__device__ __forceinline__ float rows_sum(float v) {
  u32x2 t = swap16(asu(v), asu(v)); v = asf(t[0]) + asf(t[1]);
  t = swap32(asu(v), asu(v)); return asf(t[0]) + asf(t[1]);
}
__device__ __forceinline__ void wg_barrier() { asm volatile("s_waitcnt lgkmcnt(0)\n\ts_barrier" ::: "memory"); }
__device__ __forceinline__ u32x4 granule(f32x4 v, u32x4 tg) {
  u32x4 g;
#pragma unroll
  for (int i = 0; i < 4; ++i) g[i] = ((asu(v[i]) + 0x200u) & ~0x3ffu) | tg[i];
  return g;
}
__device__ __forceinline__ bool gvalid(u32x4 g, u32x4 tg) { return ((((g[0] ^ tg[0]) | (g[1] ^ tg[1])) | ((g[2] ^ tg[2]) | (g[3] ^ tg[3]))) & 0x3ffu) == 0; }
__device__ __forceinline__ f32x4 gval(u32x4 g) { return __builtin_bit_cast(f32x4, g & ~0x3ffu); }

constexpr bool landing(int k) { return k >= 0 && k < 30 && k % 2 == 0 && k != 2; }
template <int K> __device__ __forceinline__ void ST() {
  if constexpr (landing(K)) { SB(); asm volatile(".Lst%0:\n\ts_cbranch_execz .Lrt%0" ::"n"(K)); SB(); }
}
template <int I> struct IC { static constexpr int v = I; };
template <int B, int E, class F> __device__ __forceinline__ void sfor(F&& f) {
  if constexpr (B < E) { f(IC<B>{}); sfor<B + 1, E>(f); }
}

constexpr int NU = 80, NT = 84;
constexpr int cons_end(int i) {
  if (i < N0) return 0;
  int a = (i - N0 + 1) * RATE, b = i - PW + 1;
  int m = a < b ? a : b;
  return m < NU ? m : NU;
}
constexpr int step_of(int n) { int i = 0; while (cons_end(i) <= n) ++i; return i; }

__global__ void __launch_bounds__(512, 2)
fused_attn(const char* __restrict__ wqA, const char* __restrict__ wqB, const char* __restrict__ kB, const char* __restrict__ x, char* ws,
           const char* __restrict__ kA, const char* __restrict__ gw, const char* __restrict__ vB, const char* __restrict__ vA,
           const char* __restrict__ woA, const char* __restrict__ woB, char* out) {
  int t = threadIdx.x, bid = blockIdx.x;
  typedef unsigned long long u64;
  typedef u64 u64x8 __attribute__((ext_vector_type(8)));
  typedef u64 u64x4 __attribute__((ext_vector_type(4)));
  u64x8 ka0; u64x4 ka1; u64 t64;
  const auto kp = __builtin_amdgcn_kernarg_segment_ptr();
  asm volatile(
  "s_load_dwordx16 %0, %4, 0x0\n\ts_load_dwordx8 %1, %4, 0x40\n\ts_memrealtime %3"
  : "=&s"(ka0), "=&s"(ka1), "+s"(bid), "=&s"(t64) : "s"(kp));
  SB();
  asm volatile("" : "+v"(t));
  __builtin_assume(t >= 0 && t < 512 && bid >= 0 && bid < 256);
  SB();
  unsigned pre;
  {
    unsigned long long pc; unsigned len;
    asm volatile("s_getpc_b64 %0\n1:\ts_mov_b32 %1, .Lcode_end-1b" : "=s"(pc), "=s"(len));
    pre = __builtin_amdgcn_raw_buffer_load_b32(mk((const char*)pc, len), (unsigned)(t - 448) < 8 ? (((bid >> 3) * 8 + t - 448) & 255) * 128 : OOB, 0, 0);
    SB();
  }
  __shared__ __attribute__((aligned(16))) char lds[3 * 8192 + 4096];
  const int lane = t & 63, r = lane >> 4, c = lane & 15;
  const int w = __builtin_amdgcn_readfirstlane(t >> 6);
  const int h = (bid & 7) * 4 + (bid >> 6), b = (bid >> 5) & 1, s = (bid >> 3) & 3;
  const int bh = b * 32 + h;

  const int vs = lane * 16;
  int vA_ = (bh * 4096 + s * 1024 + w * 128) * 256 + vs, vB_ = (bh * 4096 + 3072 + s * 256 + w * 32) * 256 + vs;
  int vx = (c >> 3) * 1024 + (s * 128 + w * 16 + r * 4) * 2;
  int vq = (s * 128 + w * 16 + r * 4) * 8192 + h * 256 + c * 16, vo = (h * 128 + w * 16 + r * 4) * 1024 + s * 256 + c * 16;

  const int rot = (w * 4 + s) & 31, rotb = rot & 7;
  int rb10 = rotb << 10;
  asm volatile("" : "+v"(vq), "+v"(vx), "+v"(vB_));
  asm volatile("" : "+s"(rb10));
  asm volatile("s_waitcnt lgkmcnt(0)" : "+s"(ka0), "+s"(ka1), "+s"(t64));
  wqA = (const char*)ka0[0]; wqB = (const char*)ka0[1]; kB = (const char*)ka0[2]; x = (const char*)ka0[3]; ws = (char*)ka0[4]; kA = (const char*)ka0[5]; gw = (const char*)ka0[6]; vB = (const char*)ka0[7];
  vA = (const char*)ka1[0]; woA = (const char*)ka1[1]; woB = (const char*)ka1[2]; out = (char*)ka1[3];
  unsigned tstart = (unsigned)t64;
  const rsrc_t rKA = mk(kA, 64 << 20), rKB = mk(kB, 64 << 20);
  rsrc_t rVA, rVB, rO;
  const rsrc_t rQ = mk(b ? wqB : wqA, 4 << 20);
  const rsrc_t rX = mk(x, 2048), rG = mk(gw, 1024), rW = mk(ws, WS_USED);
  u32x4 buf[NT];
  auto issue = [&](auto N) {
    constexpr int n = N.v;
    if constexpr (n >= NU) buf[n] = __builtin_amdgcn_raw_buffer_load_b128(rO, vo + (n - NU) * 1024, 0, NT_);
    else {
      constexpr int k = n % 40, u = k < 8 ? k : k - 8;
      const rsrc_t rs = n < 8 ? rKB : n < 40 ? rKA : n < 48 ? rVB : rVA;
      buf[n] = __builtin_amdgcn_raw_buffer_load_b128(rs, k < 8 ? vB_ : vA_, k < 8 ? (u * 1024) ^ rb10 : (u ^ rot) * 1024, NT_);
    }
  };
  u32x4 wq[4];

#pragma unroll
  for (int u = 0; u < 4; ++u) wq[u] = __builtin_amdgcn_raw_buffer_load_b128(rQ, vq, u * 8192, NT_);
  const u32x2 ep0 = __builtin_amdgcn_raw_buffer_load_b64(rW, 0, 0, DEV);
  const u32x2 xq = __builtin_amdgcn_raw_buffer_load_b64(rX, vx, 0, 0);
  SB();
  asm volatile("s_barrier");
  SB();
  sfor<0, I0>([&](auto I) { issue(I); SB(); });
  SB();
  rVA = mk(vA, 64 << 20); rVB = mk(vB, 64 << 20); rO = mk(b ? woB : woA, 4 << 20);
  u32x4 mask, maskA, maskB;
#pragma unroll
  for (int d = 0; d < 4; ++d) {
    mask[d] = ((c & 7) >> 1) == d ? ((c & 1) ? 0xffff0000u : 0x0000ffffu) : 0u;
    maskA[d] = c < 8 ? mask[d] : 0u;
    maskB[d] = mask[d] ^ maskA[d];
  }
  auto kdot = [&](const u32x4& k, const u32x4& q) { return dot2(k[3], q[3], dot2(k[2], q[2], dot2(k[1], q[1], dot2(k[0], q[0], 0.f)))); };
  const bool lo8 = c < 8, lo4 = (c & 4) == 0, lo32 = lane < 32;
  auto merge4 = [&](const float* d) {
    const float e0 = (lo8 ? d[0] : d[2]) + dpp<0x128>(lo8 ? d[2] : d[0]);
    const float e1 = (lo8 ? d[1] : d[3]) + dpp<0x128>(lo8 ? d[3] : d[1]);
    float f = (lo4 ? e0 : e1) + dpp<0x141>(lo4 ? e1 : e0);
    f += dpp<0xB1>(f);
    f += dpp<0x4E>(f);
    return rbf(rbf(f) * 0.08838834764831845f);
  };

  unsigned long long tag;
  u32x4 tg;
  u32x4 qA, qB, xg, gg;
  float gate2, mA, mB, lA, lB, sc, gM;
  float sA[8], sB[2], d4[4];
  unsigned pA[8], pB[2];
  u32x4 gml, gq;
  f32x4 oacc = {0.f, 0.f, 0.f, 0.f};
  bool timeout = false;
  constexpr int SPIN = 1 << 20;

  auto proj = [&](auto K, u32x2 xv, const u32x4* wv) {
    f32x4 acc = {0.f, 0.f, 0.f, 0.f};
    sfor<0, 4>([&](auto U) {
      constexpr int u = U.v;
      const unsigned rep = __builtin_amdgcn_perm(xv[u >> 1], xv[u >> 1], (u & 1) ? 0x03020302u : 0x01000100u);
      if constexpr (u < 3) ST<K.v + u>();
      acc = mfma(mask & rep, wv[u], acc);
    });
    return acc;
  };
  const int g64 = (r >> 1) * 32 + c * 2 + (r & 1);
  auto red_pub = [&](auto K, f32x4 acc, int area, int off) {
    float* pb = (float*)(lds + area * 8192);
    *(f32x4*)(pb + w * 256 + g64 * 4) = acc;
    ST<K.v>();
    wg_barrier();
    if (w == 0) {
      f32x4 v[8];
#pragma unroll
      for (int i = 0; i < 6; ++i) v[i] = *(const f32x4*)(pb + i * 256 + lane * 4);
      ST<K.v + 1>();
#pragma unroll
      for (int i = 6; i < 8; ++i) v[i] = *(const f32x4*)(pb + i * 256 + lane * 4);
      const f32x4 z0 = v[0] + v[1], z1 = v[2] + v[3];
      ST<K.v + 2>();
      const f32x4 z2 = z0 + z1, z3 = v[4] + v[5], z4 = v[6] + v[7];
      ST<K.v + 3>();
      const u32x4 z = __builtin_bit_cast(u32x4, z2 + (z3 + z4)) + 0x200u;
      ST<K.v + 4>();
      __builtin_amdgcn_raw_buffer_store_b128((z & ~0x3ffu) | tg, rW, off, 0, DEV);
    }
    ST<K.v + 5>();
  };
  auto poll = [&](u32x4& g, int off) {
    if (__builtin_expect(!__all(gvalid(g, tg)), 0)) {
      int spins = 0;
#pragma nounroll
      do { asm volatile("" ::: "memory"); g = __builtin_amdgcn_raw_buffer_load_b128(rW, off, 0, DEV); } while (!__all(gvalid(g, tg)) && ++spins < SPIN);
      if (spins >= SPIN) timeout = true;
    }
  };
  const int l5 = lane >> 5, l31 = lane & 31;
  const int q_off = Q_OFF + (bh * 256 + w * 32 + l31) * 16;
  const int ml_off = ML_OFF + bh * 512 + l31 * 16;
  float* const qb = (float*)(lds + 3 * 8192);
  auto q_partial = [&]() {
    *(u32x2*)(qb + t * 2) = u32x2{1u, 1u};
    tag = ((unsigned long long)__builtin_amdgcn_readfirstlane(ep0[1]) << 32 | (unsigned)__builtin_amdgcn_readfirstlane(ep0[0])) + 0x9E3779B97F4A7C15ull;
#pragma unroll
    for (int i = 0; i < 4; ++i) tg[i] = (unsigned)(tag >> (10 * i)) & 0x3ffu;
    red_pub(IC<-9>{}, proj(IC<-9>{}, xq, wq), 0, Q_OFF + ((((l5 * 32 + h) * 2 + b) * 4 + s) * 32 + l31) * 16);
  };
  auto q_gather = [&]() {
    poll(gq, q_off);
    if (lo32) *(f32x4*)(qb + w * 128 + lane * 4) = gval(gq);
  };
  auto q_finish = [&](int br) {
    u32x2 qp[4];
    for (;;) {
      asm volatile("" ::: "memory");
#pragma unroll
      for (int i = 0; i < 4; ++i) qp[i] = *(const u32x2*)(qb + br * 512 + i * 128 + c * 8 + r * 2);
      const u32x2 o = (qp[0] | qp[1]) | (qp[2] | qp[3]);
      if (__builtin_expect(!__any((o[0] | o[1]) & 1), 1)) break;
    }
    f32x2 q2 = __builtin_bit_cast(f32x2, qp[0]);
#pragma unroll
    for (int i = 1; i < 4; ++i) q2 += __builtin_bit_cast(f32x2, qp[i]);
    const unsigned qd = cvtpk(q2[0], q2[1]);
    const u32x2 a = swap16(qd, qd);
    const u32x2 e = swap32(a[0], a[0]), o = swap32(a[1], a[1]);
    return u32x4{e[0], o[0], e[1], o[1]};
  };
  auto gate = [&]() {
    float gs = 0.f;
#pragma unroll
    for (int d = 0; d < 4; ++d) {
      const unsigned pr = cvtpk(bf_lo(xg[d]) * bf_lo(gg[d]), bf_hi(xg[d]) * bf_hi(gg[d]));
      gs += bf_lo(pr) + bf_hi(pr);
    }
    gs += dpp<0xB1>(gs); gs += dpp<0x4E>(gs); gs += dpp<0x141>(gs); gs += dpp<0x140>(gs);
    gs = rbf(rows_sum(gs));
    const float gA = rbf(1.f / (1.f + __expf(-gs)));
    gate2 = lo32 ? gA : rbf(1.f - gA);
    asm volatile("" : "+v"(gate2));
  };
  auto sm_max = [&](auto NS, const float* sv, float& m) {
    m = sv[0];
#pragma unroll
    for (int i = 1; i < NS.v; ++i) m = fmaxf(m, sv[i]);
    m = fmaxf(m, dpp<0x124>(m)); m = fmaxf(m, dpp<0x128>(m)); m = rows_max(m);
    asm volatile("" : "+v"(m));
  };
  auto sm_exp = [&](auto NS, const float* sv, unsigned* pp, float m, float& l) {
    l = 0.f;
#pragma unroll
    for (int i = 0; i < NS.v; ++i) { const float p = __builtin_amdgcn_exp2f((sv[i] - m) * LOG2E); l += p; pp[i] = cvtpk(p, p); asm volatile("" : "+v"(pp[i])); }
    asm volatile("" : "+v"(l));
  };
  auto sm_sum = [&](float& l) { l += dpp<0x124>(l); l += dpp<0x128>(l); l = rows_sum(l); asm volatile("" : "+v"(l)); };
  auto gather_max = [&]() {
    poll(gml, ml_off);
    const f32x4 v = gval(gml);
    const float mi = lo32 ? v[0] : v[2], li = lo32 ? v[1] : v[3];
    float M = fmaxf(mi, dpp<0x121>(mi)); M = fmaxf(M, dpp<0x122>(M)); M = fmaxf(M, dpp<0x124>(M)); M = fmaxf(M, dpp<0x128>(M));
    { u32x2 tt = swap16(asu(M), asu(M)); M = fmaxf(asf(tt[0]), asf(tt[1])); }
    sc = __builtin_amdgcn_exp2f((mi - M) * LOG2E) * li;
    gM = M;
    asm volatile("" : "+v"(sc), "+v"(gM));
  };
  auto gather_scale = [&]() {
    float L = sc;
    L += dpp<0xB1>(L); L += dpp<0x4E>(L); L += dpp<0x141>(L); L += dpp<0x140>(L);
    { u32x2 tt = swap16(asu(L), asu(L)); L = asf(tt[0]) + asf(tt[1]); }
    sc = __builtin_amdgcn_exp2f(((lo32 ? mA : mB) - gM) * LOG2E) / L * gate2;
    asm volatile("" : "+v"(sc));
  };
  auto consume = [&](auto N) {
    constexpr int n = N.v;
    if constexpr (n < 40) {
      d4[n & 3] = kdot(buf[n], n < 8 ? qB : qA);
      if constexpr ((n & 3) == 3) {
        const float sv = merge4(d4);
        if constexpr (n < 8) sB[n >> 2] = sv; else sA[(n - 8) >> 2] = sv;
      }
    } else {
      constexpr int u = n < 48 ? n - 40 : n - 48;
      const unsigned pb = dppu<0x150 + 4 * (u & 3)>(n < 48 ? pB[(u >> 2) & 1] : pA[u >> 2]);
      oacc = mfma((n < 48 ? maskB : maskA) & pb, buf[n], oacc);
    }
  };

  int tn = 0;
  unsigned tb = tstart;

  constexpr int SMA = step_of(39), SMB = step_of(39) - 4 > step_of(7) ? step_of(39) - 4 : step_of(7);
  static_assert(step_of(48) > SMA + 2 && step_of(40) > SMB + 2 && step_of(8) > N0, "schedule");
  int o_put, o_get, y_put, y_get;
  unsigned touch;
  u32x4 ga;
  sfor<I0, NT - EC>([&](auto I) {
    constexpr int i = I.v;
    (void)&touch; (void)&ga; (void)&pre; (void)&tb; (void)&tn; (void)&tstart;
    if constexpr (i == QP) { q_partial(); SB(); }
    if constexpr (i >= QP) asm volatile("" ::"v"(buf[i - PW]));
    SB();
    constexpr int dt = (PACE0 + PACE * (4 + i) + (i >= NU ? (i - NU + 1) * (PACE1 - PACE) : 0)) / 10;
    if constexpr (landing(i - HOP))
      asm volatile("s_cmp_lg_u32 %0, %1\n\ts_cselect_b64 exec, -1, 0\n\ts_cbranch_execz .Lst%2\n.Lrt%2:\n\ts_mov_b64 exec, -1" ::"s"(w), "n"((i - HOP) & 7), "n"(i - HOP) : "scc");
    asm volatile("s_cmp_ge_i32 %0, %2\n\ts_cbranch_scc1 2f\n1:\ts_memrealtime s[96:97]\n\ts_waitcnt lgkmcnt(0)\n\ts_sub_u32 %0, s96, %1\n\ts_cmp_ge_i32 %0, %2\n\t"
                 "s_cbranch_scc1 2f\n\ts_sleep 1\n\ts_branch 1b\n2:" : "+s"(tn) : "s"(tb), "n"(dt) : "scc", "s96", "s97");
    SB();
    issue(I);
    if constexpr (i == QP) {
      xg = __builtin_amdgcn_raw_buffer_load_b128(rX, b * 1024 + vs, 0, 0);
      gg = __builtin_amdgcn_raw_buffer_load_b128(rG, vs, 0, 0);
    }
    if constexpr (i == QP + 1) __builtin_amdgcn_raw_buffer_store_b128(u32x4{tstart << 10, 0u, 0u, 0u} | tg, rW, bid < 8 && (unsigned)(t - 192) < 16 ? T_OFF + lane * 128 + bid * 16 : OOB, 0, DEV);
    if constexpr (i == AN) ga = __builtin_amdgcn_raw_buffer_load_b128(rW, T_OFF + (bid & 15) * 128 + (lane & 7) * 16, 0, DEV);
    if constexpr (i == QG - PW - 1) gq = __builtin_amdgcn_raw_buffer_load_b128(rW, q_off, 0, DEV);
    if constexpr (i == WT) touch = __builtin_amdgcn_raw_buffer_load_b32(rO, w == 2 && lane == 0 ? vo : OOB, 0, NT_);
    if constexpr (i == QP) asm volatile("" ::"v"(pre));
    if constexpr (i == GTH) gml = __builtin_amdgcn_raw_buffer_load_b128(rW, ml_off, 0, DEV);
    SB();
    if constexpr (i == WT + PW + 1) asm volatile("" ::"v"(touch));
    if constexpr (i == QP + PW + 1) gate();
    if constexpr (i == AN + PW + 1) {
      int d = (int)(ga[0] - (tstart << 10)) >> 10;
      d += (int)dppu<0xB1>(d); d += (int)dppu<0x4E>(d); d += (int)dppu<0x141>(d);
      d = (d >> 3) + AC;
      d = __builtin_amdgcn_readfirstlane(d > 60 ? 60 : d < -60 ? -60 : d);
      if (!__all(gvalid(ga, tg))) d = 0;
      tb += d; tn -= d;
      asm volatile("" : "+s"(tb), "+s"(tn));
    }
    if constexpr (i == QG) q_gather();
    if constexpr (i == N0 - 1) { qB = q_finish(1); asm volatile("" : "+v"(qB)); }
    if constexpr (i == N0) { qA = q_finish(0); asm volatile("" : "+v"(qA)); }
    sfor<cons_end(i - 1), cons_end(i)>(consume);
    if constexpr (i == SMB + 1) sm_max(IC<2>{}, sB, mB);
    if constexpr (i == SMB + 2) { sm_exp(IC<2>{}, sB, pB, mB, lB); sm_sum(lB); }
    if constexpr (i == SMA + 1) sm_max(IC<8>{}, sA, mA);
    if constexpr (i == SMA + 2) sm_exp(IC<8>{}, sA, pA, mA, lA);
    if constexpr (i == SMA + 3) {
      sm_sum(lA);
      __builtin_amdgcn_raw_buffer_store_b128(granule(f32x4{mA, lA, mB, lB}, tg), rW, lane == 0 ? ML_OFF + bh * 512 + s * 128 + w * 16 : OOB, 0, DEV);
    }
    if constexpr (i == GTH + PW + 1) gather_max();
    if constexpr (i == GTH + PW + 2) gather_scale();
    if constexpr (i == GTH + PW + 3) {
      o_put = O_OFF + ((((h * 2 + l5) * 32 + l31) / 4 * 8 + b * 4 + s) * 4 + l31 % 4) * 16;
      o_get = O_OFF + ((((h * 2 + b) * 32 + w * 4 + r) / 4 * 8 + (c >> 3) * 4 + (c & 3)) * 4 + (w * 4 + r) % 4) * 16;
      y_put = Y_OFF + ((l5 * 128 + s * 32 + l31) * 64 + h * 2 + b) * 16;
      y_get = Y_OFF + bid * 1024 + vs;
      asm volatile("" : "+v"(o_put), "+v"(o_get), "+v"(y_put), "+v"(y_get));
    }
    SB();
  });
  ST<0>();
  sfor<cons_end(NT - EC - 1), NU>(consume);
  red_pub(IC<3>{}, oacc * sc, 1, o_put);
  SB(); sfor<NT - EC, NT>(issue); SB();
  __builtin_amdgcn_s_sleep(ODELAY);
  SB();
  u32x4 go = __builtin_amdgcn_raw_buffer_load_b128(rW, o_get, 0, DEV);
  SB(); __builtin_amdgcn_s_sleep(OP); asm volatile("" ::: "memory"); SB();
  const u32x4 g1 = __builtin_amdgcn_raw_buffer_load_b128(rW, o_get, 0, DEV);
  SB(); __builtin_amdgcn_s_sleep(OP); asm volatile("" ::: "memory"); SB();
  const u32x4 g2 = __builtin_amdgcn_raw_buffer_load_b128(rW, o_get, 0, DEV);
  SB();
  if (!__all(gvalid(go, tg))) { asm volatile(""); ST<10>(); go = g1; if (!__all(gvalid(go, tg))) { asm volatile(""); go = g2; poll(go, o_get); } }
  ST<12>();
  f32x4 o4 = gval(go);
#pragma unroll
  for (int j = 0; j < 4; ++j) o4[j] += dpp<0xB1>(o4[j]);
  ST<14>();
#pragma unroll
  for (int j = 0; j < 4; ++j) o4[j] += dpp<0x4E>(o4[j]);
  f32x4 y = proj(IC<15>{}, u32x2{cvtpk(o4[0], o4[1]), cvtpk(o4[2], o4[3])}, buf + NU);
  if (timeout) y[0] = __builtin_nanf("");
  red_pub(IC<18>{}, y, 2, y_put);
  asm volatile("" ::"v"(g1), "v"(g2));

  if (w == 1) {
    __builtin_amdgcn_s_sleep(YDELAY);
    u32x4 g;
    int spins = 0;
#pragma nounroll
    for (;;) {
      g = __builtin_amdgcn_raw_buffer_load_b128(rW, y_get, 0, DEV);
      ST<24>();
      if (__all(gvalid(g, tg)) || ++spins >= SPIN) break;
      asm volatile("" ::: "memory");
    }
    const f32x4 a4 = gval(g);
    ST<26>();
    u32x2 u0 = swap32(asu(a4[0]), asu(a4[2])), u1 = swap32(asu(a4[1]), asu(a4[3]));
    u0 = swap16(asu(asf(u0[0]) + asf(u0[1])), asu(asf(u1[0]) + asf(u1[1])));
    float a = asf(u0[0]) + asf(u0[1]);
    a += dpp<0x121>(a);
    a += dpp<0x122>(a);
    a += dpp<0x124>(a);
    a += dpp<0x128>(a);
    ST<28>();
    if (spins >= SPIN) a = __builtin_nanf("");
    asm volatile(".Lcode_end:" : "+v"(a));
    if (c == 0) __builtin_amdgcn_raw_buffer_store_b16((unsigned short)cvtpk(a, a), mk(out, 2048), (bid * 4 + c * 4 + r) * 2, 0, DEV);
    if (bid == 0 && lane == 0) __builtin_amdgcn_raw_buffer_store_b64(u32x2{(unsigned)tag, (unsigned)(tag >> 32)}, rW, 0, 0, DEV);
  }
}

extern "C" void kernel_launch(void* const* d_in, const int* in_sizes, int n_in, void* d_out, int out_size, void* d_ws, size_t ws_size, hipStream_t stream) {
  typedef const char* P;
  fused_attn<<<256, 512, 0, stream>>>((P)d_in[3], (P)d_in[8], (P)d_in[6], (P)d_in[0], (char*)d_ws, (P)d_in[1], (P)d_in[10], (P)d_in[7], (P)d_in[2],
                                      (P)d_in[4], (P)d_in[9], (char*)d_out);
}
```
